```python
import torch

def _softmax(x, axis=-1):
    return torch.softmax(x, dim=axis)
def _gelu(x):
    return torch.nn.functional.gelu(x, approximate="tanh")
def _silu(x):
    return torch.nn.functional.silu(x)
def _sigmoid(x):
    return torch.sigmoid(x)
def _layernorm(x, axis=-1, eps=1e-5):
    m = x.mean(dim=axis, keepdim=True)
    v = x.var(dim=axis, keepdim=True, unbiased=False)
    return (x - m) / torch.sqrt(v + eps)
def _scatter_add(tgt, idx, src, axis=0):
    return tgt.clone().index_add_(axis, idx, src)
def _segment_sum(src, seg, n):
    out = torch.zeros((n,) + src.shape[1:], dtype=src.dtype)
    return out.index_add_(0, seg, src)
def _pad(x, pw):
    flat = tuple(v for pair in reversed(pw) for v in pair)
    return torch.nn.functional.pad(x, flat)

kernel_name = 'standalone__kv_cache_quant_int4_int2_kv_with_per_channel_or_per_token_scales__decode'

def setup_inputs():

    _w = (lambda a: (lambda t: t.to(torch.bfloat16) if t.is_floating_point() else t)(torch.as_tensor(a)))
    B = 1
    Sq = 1
    Skv = 65536
    Hq = 32
    Hkv = 16
    Dh = 128
    bits = 4
    G = -1
    if G <= 0 or G >= Skv:
        G = max(1, Skv // 2)
    Skv = (Skv // G) * G
    ngk = Skv // G
    qmax = (1 << bits) - 1
    return {
        "q": _w((lambda *s: torch.randn(*s, dtype=torch.bfloat16) * 1.6)(B, Hq, Sq, Dh)),
        "qk": _w(torch.randint(0, qmax + 1, (B, Hkv, Skv, Dh)).to(torch.int32)),
        "qv": _w(torch.randint(0, qmax + 1, (B, Hkv, Skv, Dh)).to(torch.int32)),
        "k_scales": _w(torch.abs((lambda *s: torch.randn(*s, dtype=torch.bfloat16) * 1.6)(B, Hkv, ngk, Dh)) * 0.05 + 0.02),
        "k_zeros": _w((lambda *s: torch.randn(*s, dtype=torch.bfloat16) * 1.6)(B, Hkv, ngk, Dh) + float(qmax) / 2.0),
        "v_scales": _w(torch.abs((lambda *s: torch.randn(*s, dtype=torch.bfloat16) * 0.05)(B, Hkv, Skv, 1)) * 0.05 + 0.02),
        "v_zeros": _w((lambda *s: torch.randn(*s, dtype=torch.bfloat16) * 0.05)(B, Hkv, Skv, 1) + float(qmax) / 2.0),
    }

def reference(q, qk, qv, k_scales, k_zeros, v_scales, v_zeros):
    B, Hq, Sq, Dh = q.shape
    Hkv, Skv = qk.shape[1], qk.shape[2]
    ngk = k_scales.shape[2]
    G = Skv // ngk
    ks = (lambda x, n, axis: torch.repeat_interleave(x, n, dim=axis))(k_scales, G, axis=2)
    kz = (lambda x, n, axis: torch.repeat_interleave(x, n, dim=axis))(k_zeros, G, axis=2)
    k = ((lambda x, dt: x.to(dt))(qk, q.dtype) - kz) * ks
    v = ((lambda x, dt: x.to(dt))(qv, q.dtype) - v_zeros) * v_scales
    rep = Hq // Hkv
    if rep > 1:
        k = (lambda x, n, axis: torch.repeat_interleave(x, n, dim=axis))(k, rep, axis=1)
        v = (lambda x, n, axis: torch.repeat_interleave(x, n, dim=axis))(v, rep, axis=1)
    scale = float(Dh) ** -0.5
    logits = torch.einsum("bhqd,bhkd->bhqk", q, k) * scale
    p = _softmax(logits, axis=-1)
    o = torch.einsum("bhqk,bhkd->bhqd", p, v)
    return o
```

```cpp
#include <hip/hip_runtime.h>
#include <hip/hip_bf16.h>
#include <stdint.h>

namespace {

constexpr int HQ = 32, HKV = 16, SKV = 65536, DH = 128, NGK = 2, GTOK = SKV / NGK;
constexpr int REP = HQ / HKV;
static_assert(REP == 2, "layout assumes two query heads per kv head");

#define NSPLIT_ 64
#define KV_NT 1
constexpr int NSPLIT = NSPLIT_;
constexpr int CHUNK = SKV / NSPLIT;
constexpr int WAVES = 4, NTHR = WAVES * 64;
constexpr int TPI = 16;
constexpr int ITERS = CHUNK / (WAVES * TPI);
static_assert(GTOK % CHUNK == 0 && CHUNK % (WAVES * TPI) == 0, "chunk must not straddle a K group");

typedef int   i32x4 __attribute__((ext_vector_type(4)));
typedef float f32x2 __attribute__((ext_vector_type(2)));
typedef float f32x4 __attribute__((ext_vector_type(4)));
typedef unsigned u32x2 __attribute__((ext_vector_type(2)));

__device__ __forceinline__ float bf2f(unsigned short b) { return __uint_as_float((unsigned)b << 16); }

template <int CTRL> __device__ __forceinline__ float dpp(float x) {
  return __builtin_bit_cast(float, __builtin_amdgcn_mov_dpp(__builtin_bit_cast(int, x), CTRL, 0xf, 0xf, true));
}
constexpr int XOR1 = 0xB1, XOR2 = 0x4E, XOR7 = 0x141, ROR8 = 0x128, ROR4 = 0x124;

__device__ __forceinline__ i32x4 ldkv(const int* p) {
  return __builtin_nontemporal_load(reinterpret_cast<const i32x4*>(p));
}

__device__ __forceinline__ float swap16_add(float a, float b) {
  auto s = __builtin_amdgcn_permlane16_swap(__float_as_uint(a), __float_as_uint(b), false, false);
  return __uint_as_float(s[0]) + __uint_as_float(s[1]);
}
__device__ __forceinline__ float xrow_max(float x) {
  auto s = __builtin_amdgcn_permlane16_swap(__float_as_uint(x), __float_as_uint(x), false, false);
  x = fmaxf(__uint_as_float(s[0]), __uint_as_float(s[1]));
  auto t = __builtin_amdgcn_permlane32_swap(__float_as_uint(x), __float_as_uint(x), false, false);
  return fmaxf(__uint_as_float(t[0]), __uint_as_float(t[1]));
}
__device__ __forceinline__ float xrow_sum(float x) {
  auto s = __builtin_amdgcn_permlane16_swap(__float_as_uint(x), __float_as_uint(x), false, false);
  x = __uint_as_float(s[0]) + __uint_as_float(s[1]);
  auto t = __builtin_amdgcn_permlane32_swap(__float_as_uint(x), __float_as_uint(x), false, false);
  return __uint_as_float(t[0]) + __uint_as_float(t[1]);
}
__device__ __forceinline__ float half_sum(float x) {
  auto t = __builtin_amdgcn_permlane32_swap(__float_as_uint(x), __float_as_uint(x), false, false);
  return __uint_as_float(t[0]) + __uint_as_float(t[1]);
}

constexpr int REC = DH + 4;

__global__ void __launch_bounds__(NTHR)
kv_split_kernel(const unsigned short* __restrict__ q, const int* __restrict__ qk, const int* __restrict__ qv,
                const unsigned short* __restrict__ ksc, const unsigned short* __restrict__ kzr,
                const unsigned short* __restrict__ vsc, const unsigned short* __restrict__ vzr,
                float* __restrict__ part) {
  __shared__ __attribute__((aligned(16))) float lds[CHUNK + WAVES * 32 + WAVES * REP * REC];
  unsigned* lsz = reinterpret_cast<unsigned*>(lds);
  float* lp = lds + CHUNK;
  float* lm = lds + CHUNK + WAVES * 32;

  const int split = blockIdx.x, head = blockIdx.y;
  const int tid = threadIdx.x, lane = tid & 63, wave = __builtin_amdgcn_readfirstlane(tid >> 6);
  const int half = lane >> 5, c = lane & 31;
  const int t0 = split * CHUNK, grp = t0 / GTOK;

  const size_t base = ((size_t)head * SKV + t0 + wave * TPI) * DH + lane * 4;
  const int* kp = qk + base;
  const int* vp = qv + base;
  i32x4 kb[8], vb[8];
#pragma unroll
  for (int j = 0; j < 8; ++j) kb[j] = ldkv(kp + j * 256);
#pragma unroll
  for (int j = 0; j < 8; ++j) vb[j] = ldkv(vp + j * 256);

  for (int i = tid; i < CHUNK / 4; i += NTHR) {
    const u32x2 s = *reinterpret_cast<const u32x2*>(vsc + (size_t)head * SKV + t0 + 4 * i);
    const u32x2 z = *reinterpret_cast<const u32x2*>(vzr + (size_t)head * SKV + t0 + 4 * i);
    typedef unsigned u32x4 __attribute__((ext_vector_type(4)));
    u32x4 w;
    w[0] = (s[0] & 0xffffu) | (z[0] << 16);
    w[1] = (s[0] >> 16) | (z[0] & 0xffff0000u);
    w[2] = (s[1] & 0xffffu) | (z[1] << 16);
    w[3] = (s[1] >> 16) | (z[1] & 0xffff0000u);
    *reinterpret_cast<u32x4*>(lsz + 4 * i) = w;
  }

  constexpr float C2 = 0.088388347648318440f * 1.4426950408889634f;
  f32x2 qs[4], sinit = {0.f, 0.f};
  {
    const u32x2 q0 = *reinterpret_cast<const u32x2*>(q + (size_t)(head * 2 + 0) * DH + 4 * c);
    const u32x2 q1 = *reinterpret_cast<const u32x2*>(q + (size_t)(head * 2 + 1) * DH + 4 * c);
    const u32x2 sk = *reinterpret_cast<const u32x2*>(ksc + (size_t)(head * NGK + grp) * DH + 4 * c);
    const u32x2 zk = *reinterpret_cast<const u32x2*>(kzr + (size_t)(head * NGK + grp) * DH + 4 * c);
#pragma unroll
    for (int i = 0; i < 4; ++i) {
      const unsigned sh = (i & 1) * 16;
      const float fq0 = __uint_as_float(((q0[i >> 1] >> sh) & 0xffffu) << 16);
      const float fq1 = __uint_as_float(((q1[i >> 1] >> sh) & 0xffffu) << 16);
      const float fs = __uint_as_float(((sk[i >> 1] >> sh) & 0xffffu) << 16) * C2;
      const float fz = __uint_as_float(((zk[i >> 1] >> sh) & 0xffffu) << 16);
      qs[i] = f32x2{fq0 * fs, fq1 * fs};
      sinit -= qs[i] * fz;
    }
  }
  __syncthreads();

  float m = -INFINITY, l = 0.f, zacc = 0.f;
  f32x2 acc[4] = {{0.f, 0.f}, {0.f, 0.f}, {0.f, 0.f}, {0.f, 0.f}};
  const int myj = ((lane >> 4) & 1) * 4 + ((lane >> 3) & 1) * 2 + ((lane >> 2) & 1);
  const int mytok = wave * TPI + 2 * myj + half;
  float* pw = lp + wave * 32;

  for (int it = 0; it < ITERS; ++it) {
    const bool more = it + 1 < ITERS;
    const int* kn = kp + (size_t)(it + 1) * (WAVES * TPI * DH);
    const int* vn = vp + (size_t)(it + 1) * (WAVES * TPI * DH);
    f32x2 s[8];
#pragma unroll
    for (int j = 0; j < 8; ++j) {
      const i32x4 x = kb[j];
      f32x2 a = sinit;
#pragma unroll
      for (int i = 0; i < 4; ++i) { const float f = (float)x[i]; a = __builtin_elementwise_fma(f32x2{f, f}, qs[i], a); }
      s[j] = a;
      if (more) kb[j] = ldkv(kn + j * 256);
    }
    const unsigned sz = lsz[it * (WAVES * TPI) + mytok];
    float r[4][2];
#pragma unroll
    for (int j = 0; j < 4; ++j) { r[j][0] = swap16_add(s[j][0], s[j + 4][0]); r[j][1] = swap16_add(s[j][1], s[j + 4][1]); }
    float u[2][2];
#pragma unroll
    for (int j = 0; j < 2; ++j)
#pragma unroll
      for (int h = 0; h < 2; ++h) {
        const float a = r[j][h] + dpp<ROR8>(r[j][h]), b = r[j + 2][h] + dpp<ROR8>(r[j + 2][h]);
        u[j][h] = (lane & 8) ? b : a;
      }
    float w[2];
#pragma unroll
    for (int h = 0; h < 2; ++h) {
      const float a = u[0][h] + dpp<XOR7>(u[0][h]), b = u[1][h] + dpp<XOR7>(u[1][h]);
      w[h] = (lane & 4) ? b : a;
    }
    float sc;
    {
      const float a = w[0] + dpp<XOR2>(w[0]), b = w[1] + dpp<XOR2>(w[1]);
      sc = (lane & 2) ? b : a;
      sc += dpp<XOR1>(sc);
    }
    float pm = fmaxf(sc, dpp<ROR4>(sc)); pm = fmaxf(pm, dpp<ROR8>(pm)); pm = xrow_max(pm);
    const float mn = fmaxf(m, pm);
    const float alpha = __builtin_amdgcn_exp2f(m - mn);
    const float p = __builtin_amdgcn_exp2f(sc - mn);
    m = mn;
    l = fmaf(l, alpha, p);
    const float pv = p * __uint_as_float(sz << 16);
    zacc = fmaf(zacc, alpha, pv * __uint_as_float(sz & 0xffff0000u));
    pw[lane >> 1] = pv;
    const f32x2 al = {dpp<0x00>(alpha), dpp<0xAA>(alpha)};
#pragma unroll
    for (int i = 0; i < 4; ++i) acc[i] *= al;
    __builtin_amdgcn_wave_barrier();
    f32x4 pr[4];
#pragma unroll
    for (int u4 = 0; u4 < 4; ++u4) pr[u4] = *reinterpret_cast<const f32x4*>(pw + half * 16 + 4 * u4);
    __builtin_amdgcn_wave_barrier();
#pragma unroll
    for (int j = 0; j < 8; ++j) {
      const i32x4 x = vb[j];
      const f32x2 pj = {pr[j >> 1][(j & 1) * 2], pr[j >> 1][(j & 1) * 2 + 1]};
#pragma unroll
      for (int i = 0; i < 4; ++i) { const float f = (float)x[i]; acc[i] = __builtin_elementwise_fma(f32x2{f, f}, pj, acc[i]); }
      if (more) vb[j] = ldkv(vn + j * 256);
    }
  }

  l += dpp<ROR4>(l); l += dpp<ROR8>(l); l = xrow_sum(l);
  zacc += dpp<ROR4>(zacc); zacc += dpp<ROR8>(zacc); zacc = xrow_sum(zacc);
  const f32x2 zz = {dpp<0x00>(zacc), dpp<0xAA>(zacc)};
#pragma unroll
  for (int i = 0; i < 4; ++i) { acc[i][0] = half_sum(acc[i][0]); acc[i][1] = half_sum(acc[i][1]); acc[i] -= zz; }
  {
    float* rec = lm + (wave * REP + half) * REC;
    *reinterpret_cast<f32x4*>(rec + 4 + 4 * c) = half ? f32x4{acc[0][1], acc[1][1], acc[2][1], acc[3][1]}
                                                      : f32x4{acc[0][0], acc[1][0], acc[2][0], acc[3][0]};
    if (lane == 0 || lane == 2) { float* r2 = lm + (wave * REP + (lane >> 1)) * REC; r2[0] = m; r2[1] = l; }
  }
  __syncthreads();
  {
    const int h = tid >> 7, d = tid & 127;
    float M = -INFINITY;
#pragma unroll
    for (int w2 = 0; w2 < WAVES; ++w2) M = fmaxf(M, lm[(w2 * REP + h) * REC]);
    float L = 0.f, o = 0.f;
#pragma unroll
    for (int w2 = 0; w2 < WAVES; ++w2) {
      const float* rec = lm + (w2 * REP + h) * REC;
      const float wt = __builtin_amdgcn_exp2f(rec[0] - M);
      L = fmaf(wt, rec[1], L); o = fmaf(wt, rec[4 + d], o);
    }
    float* dst = part + ((size_t)(head * REP + h) * NSPLIT + split) * REC;
    dst[4 + d] = o;
    if (d == 0) { dst[0] = M; dst[1] = L; }
  }
}

__global__ void __launch_bounds__(256)
kv_combine_kernel(const float* __restrict__ part, __hip_bfloat16* __restrict__ out) {
  __shared__ float so[2][DH], sl[2];
  const int hq = blockIdx.x, tid = threadIdx.x, d = tid & 127, g = tid >> 7;
  const float* p = part + (size_t)hq * NSPLIT * REC;
  float M = -INFINITY;
  for (int s = 0; s < NSPLIT; ++s) M = fmaxf(M, p[s * REC]);
  float L = 0.f, o = 0.f;
#pragma unroll 8
  for (int s = g; s < NSPLIT; s += 2) {
    const float wt = __builtin_amdgcn_exp2f(p[s * REC] - M);
    L = fmaf(wt, p[s * REC + 1], L); o = fmaf(wt, p[s * REC + 4 + d], o);
  }
  so[g][d] = o; if (d == 0) sl[g] = L;
  __syncthreads();
  if (g == 0) out[hq * DH + d] = __float2bfloat16((so[0][d] + so[1][d]) / (sl[0] + sl[1]));
}

}

extern "C" void kernel_launch(void* const* d_in, const int* in_sizes, int n_in, void* d_out, int out_size,
                              void* d_ws, size_t ws_size, hipStream_t stream) {
  (void)in_sizes; (void)n_in; (void)out_size; (void)ws_size;
  const unsigned short* q = (const unsigned short*)d_in[0];
  const int* qk = (const int*)d_in[1];
  const int* qv = (const int*)d_in[2];
  const unsigned short* ksc = (const unsigned short*)d_in[3];
  const unsigned short* kzr = (const unsigned short*)d_in[4];
  const unsigned short* vsc = (const unsigned short*)d_in[5];
  const unsigned short* vzr = (const unsigned short*)d_in[6];
  float* part = (float*)d_ws;
  kv_split_kernel<<<dim3(NSPLIT, HKV), NTHR, 0, stream>>>(q, qk, qv, ksc, kzr, vsc, vzr, part);
  kv_combine_kernel<<<HQ, 256, 0, stream>>>(part, (__hip_bfloat16*)d_out);
}
```

```cpp
#include <hip/hip_runtime.h>
#include <hip/hip_bf16.h>
#include <stdint.h>

namespace {

constexpr int HQ = 32, HKV = 16, SKV = 65536, DH = 128, NGK = 2;
static_assert(HQ / HKV == 2, "layout assumes two query heads per kv head");

constexpr int NWG = 256, SW = 4, WAVES = SW + 1, NTHR = WAVES * 64;
constexpr int GTOK = SKV / NGK;
constexpr int TPB = 16;
constexpr int NBW = GTOK / (8 * SW * TPB);
constexpr int PF = 6;
constexpr int RPH = NWG / HKV;
static_assert(NBW == 64 && PF <= 8, "geometry");

constexpr int PW0 = 13, PW1 = 14, PJ0 = 16, PJ1 = 21, PJ2 = 23;
constexpr int RMASK = 0xffe000 & ~((1 << PW0) | (1 << PW1) | (1 << PJ0) | (1 << PJ1) | (1 << PJ2));

constexpr unsigned TAU0 = 1725u, TAU_MIN = 1650u, TAU_MAX = 2400u;
constexpr int T0 = 640;
constexpr int SLACK = 5000, MARGIN = 80, PHI = 1725, RAMP = 16, PROBE = 7, RSH = 10, BARRIER_AT = 8;

constexpr int CW_STRIDE = 32;
constexpr int CW_EPOCH = 0, CW_DONE = 1, CW_HEAD = 2;
constexpr int REC_BYTES = 1088;
constexpr size_t WS_REC = 16384;
constexpr size_t WS_PACE = 1u << 20;
constexpr size_t WS_END = WS_PACE + 64 * 40 * 256;
constexpr int TAG_SHIFT = 24;
constexpr unsigned CNT_MASK = (1u << TAG_SHIFT) - 1u;

typedef int   i32x4 __attribute__((ext_vector_type(4)));
typedef float f32x2 __attribute__((ext_vector_type(2)));
typedef float f32x4 __attribute__((ext_vector_type(4)));
typedef unsigned u32x2 __attribute__((ext_vector_type(2)));
typedef unsigned u32x4 __attribute__((ext_vector_type(4)));
typedef unsigned long long u64;

template <int CTRL> __device__ __forceinline__ float dpp(float x) {
  return __builtin_bit_cast(float, __builtin_amdgcn_mov_dpp(__builtin_bit_cast(int, x), CTRL, 0xf, 0xf, true));
}
template <int CTRL> __device__ __forceinline__ unsigned dppu(unsigned x) {
  return (unsigned)__builtin_amdgcn_mov_dpp((int)x, CTRL, 0xf, 0xf, true);
}
constexpr int XOR1 = 0xB1, XOR2 = 0x4E, XOR7 = 0x141, ROR8 = 0x128, ROR4 = 0x124;

__device__ __forceinline__ float swap16_add(float a, float b) {
  auto s = __builtin_amdgcn_permlane16_swap(__float_as_uint(a), __float_as_uint(b), false, false);
  return __uint_as_float(s[0]) + __uint_as_float(s[1]);
}
__device__ __forceinline__ float xrow_max(float x) {
  auto s = __builtin_amdgcn_permlane16_swap(__float_as_uint(x), __float_as_uint(x), false, false);
  x = fmaxf(__uint_as_float(s[0]), __uint_as_float(s[1]));
  auto t = __builtin_amdgcn_permlane32_swap(__float_as_uint(x), __float_as_uint(x), false, false);
  return fmaxf(__uint_as_float(t[0]), __uint_as_float(t[1]));
}
__device__ __forceinline__ float xrow_sum(float x) {
  auto s = __builtin_amdgcn_permlane16_swap(__float_as_uint(x), __float_as_uint(x), false, false);
  x = __uint_as_float(s[0]) + __uint_as_float(s[1]);
  auto t = __builtin_amdgcn_permlane32_swap(__float_as_uint(x), __float_as_uint(x), false, false);
  return __uint_as_float(t[0]) + __uint_as_float(t[1]);
}
__device__ __forceinline__ float half_sum(float x) {
  auto t = __builtin_amdgcn_permlane32_swap(__float_as_uint(x), __float_as_uint(x), false, false);
  return __uint_as_float(t[0]) + __uint_as_float(t[1]);
}
#define SB() __builtin_amdgcn_sched_barrier(0)
#define RLX_AGENT __ATOMIC_RELAXED, __HIP_MEMORY_SCOPE_AGENT
#define RLX_WG __ATOMIC_RELAXED, __HIP_MEMORY_SCOPE_WORKGROUP
#define LDS_FENCE() asm volatile("s_waitcnt lgkmcnt(0)" ::: "memory")

constexpr int L_SZ = 0;
constexpr int L_S = L_SZ + SW * NBW * TPB;
constexpr int PREC = 2 * DH + 4;
constexpr int L_PART = L_S + SW * (NBW + 1) * 32;
constexpr int L_FLAG = L_PART + SW * PREC;
constexpr int F_DONE = 0, F_SCHED = 2  , F_PROG = 4  , F_COUNT = 4 + 2 * SW;
constexpr int LDS_FLOATS = 21 * 1024;
static_assert(L_FLAG + F_COUNT <= LDS_FLOATS && L_PART % 4 == 0 && L_S % 4 == 0 && L_FLAG % 2 == 0, "lds");

__device__ __forceinline__ int lds_ld(const float* lds, int w) {
  return __builtin_amdgcn_readfirstlane(__hip_atomic_load(reinterpret_cast<const int*>(lds + L_FLAG) + w, RLX_WG));
}
__device__ __forceinline__ u64 lds_ld2(const float* lds, int w) {
  const u64 v = __hip_atomic_load(reinterpret_cast<const u64*>(lds + L_FLAG + w), RLX_WG);
  return ((u64)(unsigned)__builtin_amdgcn_readfirstlane((int)(v >> 32)) << 32) | (unsigned)__builtin_amdgcn_readfirstlane((int)v);
}
__device__ __forceinline__ void lds_st2(float* lds, int w, unsigned lo, unsigned hi) {
  __hip_atomic_store(reinterpret_cast<u64*>(lds + L_FLAG + w), ((u64)hi << 32) | lo, RLX_WG);
}
__device__ __forceinline__ int scatter_round(int r) {
  int o = 0, mask = RMASK;
#pragma unroll
  for (int k = 0; k < 6; ++k) { const int low = mask & -mask; o |= ((r >> k) & 1) ? low : 0; mask ^= low; }
  return o;
}

__global__ void __launch_bounds__(NTHR, 1)
kv_decode_kernel(const unsigned short* __restrict__ q, const int* __restrict__ qk, const int* __restrict__ qv,
                 const unsigned short* __restrict__ ksc, const unsigned short* __restrict__ kzr,
                 const unsigned short* __restrict__ vsc, const unsigned short* __restrict__ vzr,
                 unsigned char* __restrict__ ws, __hip_bfloat16* __restrict__ out) {
  __shared__ __attribute__((aligned(16))) float lds[LDS_FLOATS];
  int* lflag = reinterpret_cast<int*>(lds + L_FLAG);

  const int tid = threadIdx.x, lane = tid & 63, wave = __builtin_amdgcn_readfirstlane(tid >> 6);
  const int half = lane >> 5, c = lane & 31;
  const int b = blockIdx.x, head = b / RPH, hg = b >> 3;
  const int wgoff = ((b & 1) << PJ0) + (((b >> 1) & 1) << PJ1) + (((b >> 2) & 1) << PJ2);

  if (__builtin_expect(wave < SW, 1)) {
    const __amdgpu_buffer_rsrc_t rk = __builtin_amdgcn_make_buffer_rsrc((void*)(qk + (size_t)hg * GTOK * DH), 0, GTOK * DH * 4, 0x00020000);
    const __amdgpu_buffer_rsrc_t rv = __builtin_amdgcn_make_buffer_rsrc((void*)(qv + (size_t)hg * GTOK * DH), 0, GTOK * DH * 4, 0x00020000);
#define LDJ(R, j, o) __builtin_bit_cast(i32x4, __builtin_amdgcn_raw_buffer_load_b128(R, (o), (j) << 10, 2  ))
    const unsigned tstart = (unsigned)__builtin_amdgcn_s_memrealtime() << 4;
    const u32x2 q0 = *reinterpret_cast<const u32x2*>(q + (head * 2 + 0) * DH + 4 * c);
    const u32x2 q1 = *reinterpret_cast<const u32x2*>(q + (head * 2 + 1) * DH + 4 * c);
    const u32x2 sk = *reinterpret_cast<const u32x2*>(ksc + hg * DH + 4 * c);
    const u32x2 zk = *reinterpret_cast<const u32x2*>(kzr + hg * DH + 4 * c);
    SB();
    const int lbase = wgoff + ((wave & 1) << PW0) + ((wave >> 1) << PW1) + lane * 16;
    int rsc = RMASK & -RMASK;
    i32x4 s0[8], sl[8];
    int oc = lbase + rsc;
#pragma unroll
    for (int j = 0; j < 8; ++j) { s0[j] = LDJ(rk, j, lbase); SB(); }
#pragma unroll
    for (int j = 0; j < PF; ++j) { sl[j] = LDJ(rk, j, oc); SB(); }

    constexpr float C2 = 0.088388347648318440f * 1.4426950408889634f;
    f32x2 qs[4], sinit = {0.f, 0.f};
#pragma unroll
    for (int i = 0; i < 4; ++i) {
      const unsigned sh = (i & 1) * 16;
      const float fq0 = __uint_as_float(((q0[i >> 1] >> sh) & 0xffffu) << 16);
      const float fq1 = __uint_as_float(((q1[i >> 1] >> sh) & 0xffffu) << 16);
      const float fs = __uint_as_float(((sk[i >> 1] >> sh) & 0xffffu) << 16) * C2;
      const float fz = __uint_as_float(((zk[i >> 1] >> sh) & 0xffffu) << 16);
      qs[i] = f32x2{fq0 * fs, fq1 * fs};
      sinit -= qs[i] * fz;
    }
    float* sw = lds + L_S + wave * ((NBW + 1) * 32);
    float m = -INFINITY;

    int phi = (PHI * (2 * (wave * 4 + ((b >> 3) & 3)) - 15)) / 32;
    const int dramp = phi / RAMP;
    unsigned origin = tstart + T0, tau = TAU0;
    unsigned now_ = 0; u64 schv = 0;
#define PACE_SAMPLE() do { SB(); now_ = (unsigned)__builtin_amdgcn_s_memrealtime() << 4; \
      schv = __hip_atomic_load(reinterpret_cast<const u64*>(lds + L_FLAG + F_SCHED), RLX_WG); SB(); } while (0)
#define PACE() do { \
      if (it > BARRIER_AT + 1) {                                   \
        const unsigned t_ = (unsigned)__builtin_amdgcn_readfirstlane((int)(schv >> 32)); \
        if (t_ != 0u) { origin = (unsigned)__builtin_amdgcn_readfirstlane((int)schv); tau = t_; } \
      } \
      if (it >= 2 * NBW - RAMP) phi -= dramp; \
      int ahead_ = (int)(origin + (unsigned)it * tau + (unsigned)phi - now_); \
      if (ahead_ > 0) { \
        ahead_ = ahead_ < 4000 ? ahead_ : 4000;                    \
        for (int d_ = (ahead_ * 5) >> 8; d_ > 0; --d_) __builtin_amdgcn_s_sleep(1); \
      } else { \
        if (ahead_ < -SLACK) origin += (unsigned)(-SLACK - ahead_); \
        ahead_ = 0; \
      } \
      if (it > BARRIER_AT && lane == 0) lds_st2(lds, F_PROG + 2 * wave, (unsigned)it, now_ + (unsigned)ahead_ - (unsigned)phi); \
    } while (0)
#define ISSUE(j, RC, RN) do { SB(); if ((j) + PF < 8) sl[((j) + PF) & 7] = LDJ(RC, ((j) + PF) & 7, oc); else sl[((j) + PF) & 7] = LDJ(RN, ((j) + PF) & 7, on); SB(); } while (0)
#define KDOT(SRC, j) do { \
        const i32x4 x = SRC[j]; \
        f32x2 a = sinit; \
        _Pragma("unroll") for (int i = 0; i < 4; ++i) { const float f = (float)x[i]; a = __builtin_elementwise_fma(f32x2{f, f}, qs[i], a); } \
        s[j] = a; \
        asm volatile("" : "+v"(s[j]));                               \
    } while (0)
    float r[4][2], u[2][2];
#define RED_A() do { _Pragma("unroll") for (int j = 0; j < 4; ++j) { r[j][0] = swap16_add(s[j][0], s[j + 4][0]); r[j][1] = swap16_add(s[j][1], s[j + 4][1]); } \
      asm volatile("" : "+v"(r[0][0]), "+v"(r[0][1]), "+v"(r[1][0]), "+v"(r[1][1]), "+v"(r[2][0]), "+v"(r[2][1]), "+v"(r[3][0]), "+v"(r[3][1])); } while (0)
#define RED_B() do { _Pragma("unroll") for (int j = 0; j < 2; ++j) \
        _Pragma("unroll") for (int h = 0; h < 2; ++h) { \
          const float a = r[j][h] + dpp<ROR8>(r[j][h]), bb = r[j + 2][h] + dpp<ROR8>(r[j + 2][h]); \
          u[j][h] = (lane & 8) ? bb : a; } \
      asm volatile("" : "+v"(u[0][0]), "+v"(u[0][1]), "+v"(u[1][0]), "+v"(u[1][1])); } while (0)
#define RED_C(blk) do { \
      float w[2]; \
      _Pragma("unroll") for (int h = 0; h < 2; ++h) { \
        const float a = u[0][h] + dpp<XOR7>(u[0][h]), bb = u[1][h] + dpp<XOR7>(u[1][h]); \
        w[h] = (lane & 4) ? bb : a; \
      } \
      const float a = w[0] + dpp<XOR2>(w[0]), bb = w[1] + dpp<XOR2>(w[1]); \
      float sc = (lane & 2) ? bb : a; \
      sc += dpp<XOR1>(sc); \
      m = fmaxf(m, sc); \
      sw[(blk) * 32 + (lane >> 1)] = sc; \
    } while (0)

    f32x2 s[8];
    SB();
#pragma unroll
    for (int j = 0; j < 8; ++j) { KDOT(s0, j); SB(); }
    RED_A();
    PACE_SAMPLE();
    for (int it = 1; it < NBW; ++it) {
      PACE();
      if (it == BARRIER_AT) asm volatile("s_barrier" ::: "memory");
      rsc = ((rsc | ~RMASK) + 1) & RMASK;
      const int on = lbase + rsc;
      const __amdgpu_buffer_rsrc_t rn = it + 1 < NBW ? rk : rv;
      SB();
#pragma unroll
      for (int j = 0; j < 8; ++j) {
        KDOT(sl, j);
        ISSUE(j, rk, rn);
        if (j == 0) { RED_B(); SB(); }
        if (j == 1) { RED_C(it - 1); SB(); }
        if (j == 5) PACE_SAMPLE();
      }
      RED_A();
      oc = on;
    }
    RED_B();
    RED_C(NBW - 1);
    m = fmaxf(m, dpp<ROR4>(m)); m = fmaxf(m, dpp<ROR8>(m)); m = xrow_max(m);

    const int myj = ((lane >> 4) & 1) * 4 + ((lane >> 3) & 1) * 2 + ((lane >> 2) & 1);
    const unsigned short* szp = reinterpret_cast<const unsigned short*>(lds + L_SZ) + (lane & 1) * (SW * NBW * TPB) + wave * (NBW * TPB) + 2 * myj + half;
    float l = 0.f, zacc = 0.f;
    f32x2 acc[4] = {{0.f, 0.f}, {0.f, 0.f}, {0.f, 0.f}, {0.f, 0.f}};
#define MAKEP(rr, LIVE) do { \
      const float sc_ = sw[(rr) * 32 + (lane >> 1)]; \
      const unsigned szc = (LIVE) ? (unsigned)szp[((rr) & (NBW - 1)) * TPB] << 16 : 0u;     \
      const float p = (LIVE) ? __builtin_amdgcn_exp2f(sc_ - m) : 0.f; \
      const unsigned szo = dppu<XOR1>(szc); \
      const float fvs = __uint_as_float((lane & 1) ? szo : szc), fvz = __uint_as_float((lane & 1) ? szc : szo); \
      const float pv = p * fvs; \
      l += p; \
      zacc = fmaf(pv, fvz, zacc); \
      sw[(rr) * 32 + (lane >> 1)] = pv; \
    } while (0)
    __builtin_amdgcn_wave_barrier();
    MAKEP(0, true);
    __builtin_amdgcn_wave_barrier();
    f32x4 pr[4];
#pragma unroll
    for (int u4 = 0; u4 < 4; ++u4) pr[u4] = *reinterpret_cast<const f32x4*>(sw + half * 16 + 4 * u4);
    for (int it = NBW; it < 2 * NBW; ++it) {
      PACE();
      const int r0 = it - NBW;
      rsc = ((rsc | ~RMASK) + 1) & RMASK;
      const int on = (r0 + 1 < NBW ? rsc : 0x40000000) + lbase;
      SB();
#pragma unroll
      for (int j = 0; j < 8; ++j) {
        const i32x4 x = sl[j];
        const f32x2 pj = {pr[j >> 1][(j & 1) * 2], pr[j >> 1][(j & 1) * 2 + 1]};
#pragma unroll
        for (int i = 0; i < 4; ++i) { const float f = (float)x[i]; acc[i] = __builtin_elementwise_fma(f32x2{f, f}, pj, acc[i]); }
        asm volatile("" : "+v"(acc[0]), "+v"(acc[1]), "+v"(acc[2]), "+v"(acc[3]));
        ISSUE(j, rv, rv);
        if (j == 0) { MAKEP(r0 + 1, r0 + 1 < NBW); __builtin_amdgcn_wave_barrier(); SB(); }
        if (j & 1) { pr[j >> 1] = *reinterpret_cast<const f32x4*>(sw + (r0 + 1) * 32 + half * 16 + 4 * (j >> 1)); SB(); }
        if (j == 5) PACE_SAMPLE();
      }
      oc = on;
    }

    l += dpp<ROR4>(l); l += dpp<ROR8>(l); l = xrow_sum(l);
    zacc += dpp<ROR4>(zacc); zacc += dpp<ROR8>(zacc); zacc = xrow_sum(zacc);
    const f32x2 zz = {dpp<0x00>(zacc), dpp<0xAA>(zacc)};
#pragma unroll
    for (int i = 0; i < 4; ++i) { acc[i][0] = half_sum(acc[i][0]); acc[i][1] = half_sum(acc[i][1]); acc[i] -= zz; }
    float* rec = lds + L_PART + wave * PREC;
    *reinterpret_cast<f32x4*>(rec + 4 * lane) = half ? f32x4{acc[0][1], acc[1][1], acc[2][1], acc[3][1]}
                                                     : f32x4{acc[0][0], acc[1][0], acc[2][0], acc[3][0]};
    if (lane == 0 || lane == 2) *reinterpret_cast<f32x2*>(rec + 2 * DH + lane) = f32x2{m, l};
    LDS_FENCE();
    if (lane == 0) __hip_atomic_fetch_add(lflag + F_DONE, 1, RLX_WG);
    return;
  }

  u64* cw = reinterpret_cast<u64*>(ws);
  unsigned* cw32 = reinterpret_cast<unsigned*>(ws);
  {
    u32x4 sv[16];
    for (int i = 0; i < 24; ++i) __builtin_amdgcn_s_sleep(4);
#pragma unroll
    for (int i = 0; i < 8; ++i) {
      const int e = i * 64 + lane, w2 = (e >> 1) & 3, r2 = e >> 3;
      const int boff = wgoff + ((w2 & 1) << PW0) + ((w2 >> 1) << PW1) + scatter_round(r2);
      const size_t row = (size_t)hg * GTOK + (boff >> 9) + (e & 1) * 8;
      sv[2 * i] = __builtin_nontemporal_load(reinterpret_cast<const u32x4*>(vsc + row));
      sv[2 * i + 1] = __builtin_nontemporal_load(reinterpret_cast<const u32x4*>(vzr + row));
    }
    if (lane < F_COUNT) lflag[lane] = 0;
#pragma unroll
    for (int i = 0; i < 8; ++i) {
      const int e = i * 64 + lane, w2 = (e >> 1) & 3, r2 = e >> 3;
      const int d = (w2 * NBW + r2) * 2 + (e & 1);
      reinterpret_cast<u32x4*>(lds + L_SZ)[d] = sv[2 * i];
      reinterpret_cast<u32x4*>(lds + L_SZ)[SW * NBW * 2 + d] = sv[2 * i + 1];
    }
    LDS_FENCE();
    asm volatile("s_barrier" ::: "memory");
  }
  u64 epoch = 0;
  {
    const u64 c0 = __builtin_amdgcn_s_memrealtime();
    const u64 hs_time = c0 + 86u * (unsigned)((b & 15) * 4 + (b >> 6));
    bool hs_done = false;
    u64 rnd = (c0 >> RSH) + 1;
    int phase = 0;
    int kprev = -1; unsigned tprev = 0;
    unsigned tau = TAU0, origin = 0, h0 = TAU0, h1 = TAU0, h2 = TAU0;
    u64* tab = reinterpret_cast<u64*>(ws + WS_PACE);
    for (;;) {
      const bool ending = lds_ld(lds, F_PROG) >= 2 * NBW - 4;
      const u64 now = __builtin_amdgcn_s_memrealtime();
      if (!hs_done && (ending || now >= hs_time)) {
        const int wi = lane < 2 ? lane : CW_HEAD + head;
        u64 cwv = 0;
        if (lane < 3) cwv = __hip_atomic_load(cw + wi * CW_STRIDE, RLX_AGENT);
        epoch = ((u64)(unsigned)__builtin_amdgcn_readfirstlane((int)(cwv >> 32)) << 32) | (unsigned)__builtin_amdgcn_readfirstlane((int)cwv);
        const u64 tagv = (epoch * 0x9E3779B97F4A7C15ull + 0x632BE59BD9B4E019ull) << TAG_SHIFT;
        if (lane >= 1 && lane < 3 && (cwv >> TAG_SHIFT) != (tagv >> TAG_SHIFT))
          __hip_atomic_compare_exchange_strong(cw + wi * CW_STRIDE, &cwv, tagv, __ATOMIC_RELAXED, RLX_AGENT);
        asm volatile("s_waitcnt vmcnt(0)" ::: "memory");
        hs_done = true;
        continue;
      }
      if (ending) break;
      const u64 tb = rnd << RSH;
      u64* row = tab + (rnd & 63) * (40 * 32);
      if (phase == 0 && now >= tb) {
        if (b < 32) {
          unsigned ks = 0; int ts = 0; bool ok = true;
#pragma unroll
          for (int w2 = 0; w2 < SW; ++w2) {
            const u64 pg = lds_ld2(lds, F_PROG + 2 * w2);
            const int dt = (int)((unsigned)(pg >> 32) - ((unsigned)tb << 4));
            ok = ok && (unsigned)pg != 0u && dt > -30000 && dt < 30000;
            ks += (unsigned)pg; ts += dt;
          }
          if (ok && lane == 0)
            __hip_atomic_store(row + b * 32, (rnd << 28) | ((u64)(ks & 0x3ffu) << 18) | (u64)((unsigned)(ts + 131072) & 0x3ffffu), RLX_AGENT);
        }
        phase = 1;
      } else if (phase == 1 && now >= tb + 130) {
        if (b == 0) {
          u64 e = 0;
          if (lane < 32) e = __hip_atomic_load(row + lane * 32, RLX_AGENT);
          const bool val = lane < 32 && (e >> 28) == (rnd & 0xfffffffffull);
          const int n = __builtin_popcountll(__ballot(val));
          if (n >= 24) {
            int ks = val ? (int)((e >> 18) & 0x3ffu) : 0, ts = val ? (int)(e & 0x3ffffu) - 131072 : 0;
#pragma unroll
            for (int o = 1; o < 32; o <<= 1) { ks += __shfl_xor(ks, o); ts += __shfl_xor(ts, o); }
            ks = __builtin_amdgcn_readfirstlane(ks); ts = __builtin_amdgcn_readfirstlane(ts);
            const int kbar = (ks * 64) / (n * SW);
            const unsigned tbar = ((unsigned)tb << 4) + (unsigned)(ts / (n * SW));
            if (kprev >= 0 && kbar > kprev + 128) {
              unsigned slope = (unsigned)(((u64)(tbar - tprev) * 64u) / (unsigned)(kbar - kprev));
              const int lag = (int)(tbar - origin - (unsigned)(((u64)tau * (unsigned)kbar) >> 6));
              if (lag < MARGIN / 3) slope -= slope >> PROBE;
              h2 = h1; h1 = h0; h0 = slope;
              tau = min(h0, min(h1, h2));
              tau = tau < TAU_MIN ? TAU_MIN : tau > TAU_MAX ? TAU_MAX : tau;
            }
            kprev = kbar; tprev = tbar;
            origin = tbar - (unsigned)(((u64)tau * (unsigned)kbar) >> 6) - MARGIN;
            if (lane < 8) __hip_atomic_store(row + (32 + lane) * 32, ((rnd & 0xfffffull) << 44) | ((u64)tau << 32) | origin, RLX_AGENT);
          }
        }
        phase = 2;
      } else if (phase == 2 && now >= tb + 330) {
        const u64 e = __hip_atomic_load(row + (32 + (b & 7)) * 32, RLX_AGENT);
        const unsigned eo = (unsigned)e, et = (unsigned)(e >> 32) & 0xfffu;
        const int off = (int)(eo + (unsigned)lds_ld(lds, F_PROG) * et - ((unsigned)now << 4));
        if ((e >> 44) == (rnd & 0xfffffull) && et >= TAU_MIN && et <= TAU_MAX && off > -40000 && off < 40000 && lane == 0) lds_st2(lds, F_SCHED, eo, et);
        phase = 0; ++rnd;
      } else __builtin_amdgcn_s_sleep(1);
    }
    while (lds_ld(lds, F_DONE) != SW) {}
  }
  LDS_FENCE();
  const __amdgpu_buffer_rsrc_t rr = __builtin_amdgcn_make_buffer_rsrc((void*)(ws + WS_REC + (size_t)head * RPH * REC_BYTES), 0, RPH * REC_BYTES, 0x00020000);
  {
    const float* rb = lds + L_PART;
    float mw[SW], M = -INFINITY;
#pragma unroll
    for (int w2 = 0; w2 < SW; ++w2) { mw[w2] = rb[w2 * PREC + 2 * DH + 2 * half]; M = fmaxf(M, mw[w2]); }
    float L = 0.f; f32x4 o = {0.f, 0.f, 0.f, 0.f};
#pragma unroll
    for (int w2 = 0; w2 < SW; ++w2) {
      const float wt = __builtin_amdgcn_exp2f(mw[w2] - M);
      L = fmaf(wt, rb[w2 * PREC + 2 * DH + 2 * half + 1], L);
      o += wt * *reinterpret_cast<const f32x4*>(rb + w2 * PREC + 4 * lane);
    }
    const int ro = (b % RPH) * REC_BYTES;
    __builtin_amdgcn_raw_buffer_store_b128(__builtin_bit_cast(u32x4, o), rr, ro + lane * 16, 0, 16  );
    if (c == 0) __builtin_amdgcn_raw_buffer_store_b64(__builtin_bit_cast(u32x2, f32x2{M, L}), rr, ro + 1024 + half * 8, 0, 16);
  }
  asm volatile("s_waitcnt vmcnt(0)" ::: "memory");
  unsigned old = 0;
  if (lane == 0) old = __hip_atomic_fetch_add(cw32 + (CW_HEAD + head) * CW_STRIDE * 2, 1u, RLX_AGENT);
  if (((unsigned)__builtin_amdgcn_readfirstlane((int)old) & CNT_MASK) == RPH - 1) {
    int zoff = 0;
    asm volatile("" : "+v"(zoff) :: "memory");
    unsigned od = 0;
    if (lane == 0) od = __hip_atomic_fetch_add(cw32 + CW_DONE * CW_STRIDE * 2 + zoff, 1u, RLX_AGENT);
    f32x4 ov[RPH]; f32x2 ml[RPH];
#pragma unroll
    for (int i = 0; i < RPH; ++i) {
      ml[i] = __builtin_bit_cast(f32x2, __builtin_amdgcn_raw_buffer_load_b64(rr, i * REC_BYTES + 1024 + half * 8, 0, 16));
      ov[i] = __builtin_bit_cast(f32x4, __builtin_amdgcn_raw_buffer_load_b128(rr, i * REC_BYTES + lane * 16, 0, 16));
    }
    float M = -INFINITY;
#pragma unroll
    for (int i = 0; i < RPH; ++i) M = fmaxf(M, ml[i][0]);
    float L = 0.f; f32x4 o = {0.f, 0.f, 0.f, 0.f};
#pragma unroll
    for (int i = 0; i < RPH; ++i) {
      const float wt = __builtin_amdgcn_exp2f(ml[i][0] - M);
      L = fmaf(wt, ml[i][1], L);
      o += wt * ov[i];
    }
    const float inv = 1.0f / L;
    __hip_bfloat16 r4[4];
#pragma unroll
    for (int i = 0; i < 4; ++i) r4[i] = __float2bfloat16(o[i] * inv);
    *reinterpret_cast<u32x2*>(out + (head * 2 + half) * DH + 4 * c) = *reinterpret_cast<const u32x2*>(r4);
    asm volatile("" : "+v"(od));
    if (lane == 0 && (od & CNT_MASK) == HKV - 1) __hip_atomic_store(cw + CW_EPOCH * CW_STRIDE, epoch + 1, RLX_AGENT);
  }
}

}

extern "C" void kernel_launch(void* const* d_in, const int* in_sizes, int n_in, void* d_out, int out_size,
                              void* d_ws, size_t ws_size, hipStream_t stream) {
  if (n_in != 7 || in_sizes[0] != HQ * DH || in_sizes[1] != HKV * SKV * DH || in_sizes[2] != HKV * SKV * DH ||
      in_sizes[3] != HKV * NGK * DH || in_sizes[4] != HKV * NGK * DH || in_sizes[5] != HKV * SKV || in_sizes[6] != HKV * SKV ||
      out_size != HQ * DH || ws_size < WS_END)
    return;
  kv_decode_kernel<<<NWG, NTHR, 0, stream>>>((const unsigned short*)d_in[0], (const int*)d_in[1], (const int*)d_in[2],
                                             (const unsigned short*)d_in[3], (const unsigned short*)d_in[4],
                                             (const unsigned short*)d_in[5], (const unsigned short*)d_in[6],
                                             (unsigned char*)d_ws, (__hip_bfloat16*)d_out);
}
```

```cpp
#include <hip/hip_runtime.h>
#include <hip/hip_bf16.h>
#include <stdint.h>

namespace {

constexpr int HQ = 32, HKV = 16, SKV = 65536, DH = 128, NGK = 2;
static_assert(HQ / HKV == 2, "layout assumes two query heads per kv head");

constexpr int NWG = 256, SW = 4, WAVES = SW + 1, NTHR = WAVES * 64;
constexpr int GTOK = SKV / NGK;
constexpr int TPB = 16;
constexpr int NBW = GTOK / (8 * SW * TPB);
constexpr int PF = 6;
constexpr int RPH = NWG / HKV;
static_assert(NBW == 64 && PF <= 8, "geometry");

constexpr int PW0 = 13, PW1 = 14, PJ0 = 16, PJ1 = 21, PJ2 = 23;
constexpr int RMASK = 0xffe000 & ~((1 << PW0) | (1 << PW1) | (1 << PJ0) | (1 << PJ1) | (1 << PJ2));

constexpr unsigned TAU0 = 1725u, TAU_MIN = 1650u, TAU_MAX = 2400u;
constexpr int T0 = 1900;
constexpr int SLACK = 5000, MARGIN = 80, PHI = 1725, RAMP = 16, PROBE = 7, RSH = 10;
constexpr int ENDN = 40, ENDE = 20;

constexpr int CW_STRIDE = 32;
constexpr int CW_EPOCH = 0, CW_DONE = 1, CW_HEAD = 2;
constexpr int REC_BYTES = 1088;
constexpr size_t WS_REC = 16384;
constexpr size_t WS_PACE = 1u << 20;
constexpr size_t WS_END = WS_PACE + 64 * 40 * 256;
constexpr int TAG_SHIFT = 24;
constexpr unsigned CNT_MASK = (1u << TAG_SHIFT) - 1u;

typedef int   i32x4 __attribute__((ext_vector_type(4)));
typedef float f32x2 __attribute__((ext_vector_type(2)));
typedef float f32x4 __attribute__((ext_vector_type(4)));
typedef unsigned u32x2 __attribute__((ext_vector_type(2)));
typedef unsigned u32x4 __attribute__((ext_vector_type(4)));
typedef unsigned long long u64;

template <int CTRL> __device__ __forceinline__ float dpp(float x) {
  return __builtin_bit_cast(float, __builtin_amdgcn_mov_dpp(__builtin_bit_cast(int, x), CTRL, 0xf, 0xf, true));
}
template <int CTRL> __device__ __forceinline__ unsigned dppu(unsigned x) {
  return (unsigned)__builtin_amdgcn_mov_dpp((int)x, CTRL, 0xf, 0xf, true);
}
constexpr int XOR1 = 0xB1, XOR2 = 0x4E, XOR7 = 0x141, ROR8 = 0x128, ROR4 = 0x124;

__device__ __forceinline__ float swap16_add(float a, float b) {
  auto s = __builtin_amdgcn_permlane16_swap(__float_as_uint(a), __float_as_uint(b), false, false);
  return __uint_as_float(s[0]) + __uint_as_float(s[1]);
}
__device__ __forceinline__ float xrow_max(float x) {
  auto s = __builtin_amdgcn_permlane16_swap(__float_as_uint(x), __float_as_uint(x), false, false);
  x = fmaxf(__uint_as_float(s[0]), __uint_as_float(s[1]));
  auto t = __builtin_amdgcn_permlane32_swap(__float_as_uint(x), __float_as_uint(x), false, false);
  return fmaxf(__uint_as_float(t[0]), __uint_as_float(t[1]));
}
__device__ __forceinline__ float xrow_sum(float x) {
  auto s = __builtin_amdgcn_permlane16_swap(__float_as_uint(x), __float_as_uint(x), false, false);
  x = __uint_as_float(s[0]) + __uint_as_float(s[1]);
  auto t = __builtin_amdgcn_permlane32_swap(__float_as_uint(x), __float_as_uint(x), false, false);
  return __uint_as_float(t[0]) + __uint_as_float(t[1]);
}
__device__ __forceinline__ float half_sum(float x) {
  auto t = __builtin_amdgcn_permlane32_swap(__float_as_uint(x), __float_as_uint(x), false, false);
  return __uint_as_float(t[0]) + __uint_as_float(t[1]);
}
#define SB() __builtin_amdgcn_sched_barrier(0)
#define RLX_AGENT __ATOMIC_RELAXED, __HIP_MEMORY_SCOPE_AGENT
#define RLX_WG __ATOMIC_RELAXED, __HIP_MEMORY_SCOPE_WORKGROUP
#define LDS_FENCE() asm volatile("s_waitcnt lgkmcnt(0)" ::: "memory")

constexpr int L_SZ = 0;
constexpr int L_S = L_SZ + SW * NBW * TPB;
constexpr int PREC = 2 * DH + 4;
constexpr int L_PART = L_S + SW * (NBW + 1) * 32;
constexpr int L_FLAG = L_PART + SW * PREC;
constexpr int F_DONE = 0, F_STAGED = 1, F_SCHED = 2  , F_PROG = 4  , F_COUNT = 4 + 2 * SW;
constexpr int LDS_FLOATS = 21 * 1024;
static_assert(L_FLAG + F_COUNT <= LDS_FLOATS && L_PART % 4 == 0 && L_S % 4 == 0 && L_FLAG % 2 == 0, "lds");

__device__ __forceinline__ int lds_ld(const float* lds, int w) {
  return __builtin_amdgcn_readfirstlane(__hip_atomic_load(reinterpret_cast<const int*>(lds + L_FLAG) + w, RLX_WG));
}
__device__ __forceinline__ u64 lds_ld2(const float* lds, int w) {
  const u64 v = __hip_atomic_load(reinterpret_cast<const u64*>(lds + L_FLAG + w), RLX_WG);
  return ((u64)(unsigned)__builtin_amdgcn_readfirstlane((int)(v >> 32)) << 32) | (unsigned)__builtin_amdgcn_readfirstlane((int)v);
}
__device__ __forceinline__ void lds_st2(float* lds, int w, unsigned lo, unsigned hi) {
  __hip_atomic_store(reinterpret_cast<u64*>(lds + L_FLAG + w), ((u64)hi << 32) | lo, RLX_WG);
}
__device__ __forceinline__ int scatter_round(int r) {
  int o = 0, mask = RMASK;
#pragma unroll
  for (int k = 0; k < 6; ++k) { const int low = mask & -mask; o |= ((r >> k) & 1) ? low : 0; mask ^= low; }
  return o;
}

__global__ void __launch_bounds__(NTHR, 1)
kv_decode_kernel(const unsigned short* __restrict__ q, const int* __restrict__ qk, const int* __restrict__ qv,
                 const unsigned short* __restrict__ ksc, const unsigned short* __restrict__ kzr,
                 const unsigned short* __restrict__ vsc, const unsigned short* __restrict__ vzr,
                 unsigned char* __restrict__ ws, __hip_bfloat16* __restrict__ out) {
  __shared__ __attribute__((aligned(16))) float lds[LDS_FLOATS];
  int* lflag = reinterpret_cast<int*>(lds + L_FLAG);

  const int tid = threadIdx.x, lane = tid & 63, wave = __builtin_amdgcn_readfirstlane(tid >> 6);
  const int half = lane >> 5, c = lane & 31;
  const int b = blockIdx.x, head = b / RPH, hg = b >> 3;
  const int wgoff = ((b & 1) << PJ0) + (((b >> 1) & 1) << PJ1) + (((b >> 2) & 1) << PJ2);

  if (__builtin_expect(wave < SW, 1)) {
    const __amdgpu_buffer_rsrc_t rk = __builtin_amdgcn_make_buffer_rsrc((void*)(qk + (size_t)hg * GTOK * DH), 0, GTOK * DH * 4, 0x00020000);
    const __amdgpu_buffer_rsrc_t rv = __builtin_amdgcn_make_buffer_rsrc((void*)(qv + (size_t)hg * GTOK * DH), 0, GTOK * DH * 4, 0x00020000);
#define LDJ(R, j, o) __builtin_bit_cast(i32x4, __builtin_amdgcn_raw_buffer_load_b128(R, (o), (j) << 10, 2  ))
    const unsigned tstart = (unsigned)__builtin_amdgcn_s_memrealtime() << 4;
    const u32x2 q0 = *reinterpret_cast<const u32x2*>(q + (head * 2 + 0) * DH + 4 * c);
    const u32x2 q1 = *reinterpret_cast<const u32x2*>(q + (head * 2 + 1) * DH + 4 * c);
    const u32x2 sk = *reinterpret_cast<const u32x2*>(ksc + hg * DH + 4 * c);
    const u32x2 zk = *reinterpret_cast<const u32x2*>(kzr + hg * DH + 4 * c);
    SB();
    const int lbase = wgoff + ((wave & 1) << PW0) + ((wave >> 1) << PW1) + lane * 16;
    int rsc = RMASK & -RMASK;
    i32x4 s0[8], sl[8];
    int oc = lbase + rsc;
#pragma unroll
    for (int j = 0; j < 8; ++j) { s0[j] = LDJ(rk, j, lbase); SB(); }
#pragma unroll
    for (int j = 0; j < PF; ++j) { sl[j] = LDJ(rk, j, oc); SB(); }
    asm volatile("s_barrier" ::: "memory");
    SB();

    constexpr float C2 = 0.088388347648318440f * 1.4426950408889634f;
    f32x2 qs[4], sinit = {0.f, 0.f};
#pragma unroll
    for (int i = 0; i < 4; ++i) {
      const unsigned sh = (i & 1) * 16;
      const float fq0 = __uint_as_float(((q0[i >> 1] >> sh) & 0xffffu) << 16);
      const float fq1 = __uint_as_float(((q1[i >> 1] >> sh) & 0xffffu) << 16);
      const float fs = __uint_as_float(((sk[i >> 1] >> sh) & 0xffffu) << 16) * C2;
      const float fz = __uint_as_float(((zk[i >> 1] >> sh) & 0xffffu) << 16);
      qs[i] = f32x2{fq0 * fs, fq1 * fs};
      sinit -= qs[i] * fz;
    }
    float* sw = lds + L_S + wave * ((NBW + 1) * 32);
    float m = -INFINITY;

    int phi = (PHI * (2 * (wave * 4 + ((b >> 3) & 3)) - 15)) / 32;
    const int dramp = phi / RAMP;
    unsigned origin = tstart + T0, tau = TAU0;
    unsigned now_ = 0; u64 schv = 0;
#define PACE_SAMPLE() do { SB(); now_ = (unsigned)__builtin_amdgcn_s_memrealtime() << 4; \
      schv = __hip_atomic_load(reinterpret_cast<const u64*>(lds + L_FLAG + F_SCHED), RLX_WG); SB(); } while (0)
#define PACE() do { \
      { \
        const unsigned t_ = (unsigned)__builtin_amdgcn_readfirstlane((int)(schv >> 32)); \
        if (t_ != 0u) { origin = (unsigned)__builtin_amdgcn_readfirstlane((int)schv); tau = t_; } \
      } \
      if (it >= 2 * NBW - RAMP) phi -= dramp; \
      if (it >= 2 * NBW - ENDN) phi += ENDE; \
      int ahead_ = (int)(origin + (unsigned)it * tau + (unsigned)phi - now_); \
      if (ahead_ > 0) { \
        ahead_ = ahead_ < 4000 ? ahead_ : 4000;                    \
        for (int d_ = (ahead_ * 5) >> 8; d_ > 0; --d_) __builtin_amdgcn_s_sleep(1); \
      } else { \
        if (ahead_ < -SLACK) origin += (unsigned)(-SLACK - ahead_); \
        ahead_ = 0; \
      } \
      if (it >= 2 && lane == 0) lds_st2(lds, F_PROG + 2 * wave, (unsigned)it, now_ + (unsigned)ahead_ - (unsigned)phi); \
    } while (0)
#define ISSUE(j, RC, RN) do { SB(); if ((j) + PF < 8) sl[((j) + PF) & 7] = LDJ(RC, ((j) + PF) & 7, oc); else sl[((j) + PF) & 7] = LDJ(RN, ((j) + PF) & 7, on); SB(); } while (0)
#define KDOT(SRC, j) do { \
        const i32x4 x = SRC[j]; \
        f32x2 a = sinit; \
        _Pragma("unroll") for (int i = 0; i < 4; ++i) { const float f = (float)x[i]; a = __builtin_elementwise_fma(f32x2{f, f}, qs[i], a); } \
        s[j] = a; \
        asm volatile("" : "+v"(s[j]));                               \
    } while (0)
    float r[4][2], u[2][2];
#define RED_A() do { _Pragma("unroll") for (int j = 0; j < 4; ++j) { r[j][0] = swap16_add(s[j][0], s[j + 4][0]); r[j][1] = swap16_add(s[j][1], s[j + 4][1]); } \
      asm volatile("" : "+v"(r[0][0]), "+v"(r[0][1]), "+v"(r[1][0]), "+v"(r[1][1]), "+v"(r[2][0]), "+v"(r[2][1]), "+v"(r[3][0]), "+v"(r[3][1])); } while (0)
#define RED_B() do { _Pragma("unroll") for (int j = 0; j < 2; ++j) \
        _Pragma("unroll") for (int h = 0; h < 2; ++h) { \
          const float a = r[j][h] + dpp<ROR8>(r[j][h]), bb = r[j + 2][h] + dpp<ROR8>(r[j + 2][h]); \
          u[j][h] = (lane & 8) ? bb : a; } \
      asm volatile("" : "+v"(u[0][0]), "+v"(u[0][1]), "+v"(u[1][0]), "+v"(u[1][1])); } while (0)
#define RED_C(blk) do { \
      float w[2]; \
      _Pragma("unroll") for (int h = 0; h < 2; ++h) { \
        const float a = u[0][h] + dpp<XOR7>(u[0][h]), bb = u[1][h] + dpp<XOR7>(u[1][h]); \
        w[h] = (lane & 4) ? bb : a; \
      } \
      const float a = w[0] + dpp<XOR2>(w[0]), bb = w[1] + dpp<XOR2>(w[1]); \
      float sc = (lane & 2) ? bb : a; \
      sc += dpp<XOR1>(sc); \
      m = fmaxf(m, sc); \
      sw[(blk) * 32 + (lane >> 1)] = sc; \
    } while (0)

    f32x2 s[8];
    SB();
#pragma unroll
    for (int j = 0; j < 8; ++j) { KDOT(s0, j); SB(); }
    RED_A();
    PACE_SAMPLE();
    for (int it = 1; it < NBW; ++it) {
      PACE();
      rsc = ((rsc | ~RMASK) + 1) & RMASK;
      const int on = lbase + rsc;
      const __amdgpu_buffer_rsrc_t rn = it + 1 < NBW ? rk : rv;
      SB();
#pragma unroll
      for (int j = 0; j < 8; ++j) {
        KDOT(sl, j);
        ISSUE(j, rk, rn);
        if (j == 0) { RED_B(); SB(); }
        if (j == 1) { RED_C(it - 1); SB(); }
        if (j == 5) PACE_SAMPLE();
      }
      RED_A();
      oc = on;
    }
    RED_B();
    RED_C(NBW - 1);
    m = fmaxf(m, dpp<ROR4>(m)); m = fmaxf(m, dpp<ROR8>(m)); m = xrow_max(m);

    const int myj = ((lane >> 4) & 1) * 4 + ((lane >> 3) & 1) * 2 + ((lane >> 2) & 1);
    const unsigned short* szp = reinterpret_cast<const unsigned short*>(lds + L_SZ) + (lane & 1) * (SW * NBW * TPB) + wave * (NBW * TPB) + 2 * myj + half;
    float l = 0.f, zacc = 0.f;
    f32x2 acc[4] = {{0.f, 0.f}, {0.f, 0.f}, {0.f, 0.f}, {0.f, 0.f}};
#define MAKEP(rr, LIVE) do { \
      const float sc_ = sw[(rr) * 32 + (lane >> 1)]; \
      const unsigned szc = (LIVE) ? (unsigned)szp[((rr) & (NBW - 1)) * TPB] << 16 : 0u;     \
      const float p = (LIVE) ? __builtin_amdgcn_exp2f(sc_ - m) : 0.f; \
      const unsigned szo = dppu<XOR1>(szc); \
      const float fvs = __uint_as_float((lane & 1) ? szo : szc), fvz = __uint_as_float((lane & 1) ? szc : szo); \
      const float pv = p * fvs; \
      l += p; \
      zacc = fmaf(pv, fvz, zacc); \
      sw[(rr) * 32 + (lane >> 1)] = pv; \
    } while (0)
    while (lds_ld(lds, F_STAGED) == 0) __builtin_amdgcn_s_sleep(1);
    LDS_FENCE();
    MAKEP(0, true);
    __builtin_amdgcn_wave_barrier();
    f32x4 pr[4];
#pragma unroll
    for (int u4 = 0; u4 < 4; ++u4) pr[u4] = *reinterpret_cast<const f32x4*>(sw + half * 16 + 4 * u4);
    for (int it = NBW; it < 2 * NBW; ++it) {
      PACE();
      const int r0 = it - NBW;
      rsc = ((rsc | ~RMASK) + 1) & RMASK;
      const int on = (r0 + 1 < NBW ? rsc : 0x40000000) + lbase;
      SB();
#pragma unroll
      for (int j = 0; j < 8; ++j) {
        const i32x4 x = sl[j];
        const f32x2 pj = {pr[j >> 1][(j & 1) * 2], pr[j >> 1][(j & 1) * 2 + 1]};
#pragma unroll
        for (int i = 0; i < 4; ++i) { const float f = (float)x[i]; acc[i] = __builtin_elementwise_fma(f32x2{f, f}, pj, acc[i]); }
        asm volatile("" : "+v"(acc[0]), "+v"(acc[1]), "+v"(acc[2]), "+v"(acc[3]));
        ISSUE(j, rv, rv);
        if (j == 0) { MAKEP(r0 + 1, r0 + 1 < NBW); __builtin_amdgcn_wave_barrier(); SB(); }
        if (j & 1) { pr[j >> 1] = *reinterpret_cast<const f32x4*>(sw + (r0 + 1) * 32 + half * 16 + 4 * (j >> 1)); SB(); }
        if (j == 5) PACE_SAMPLE();
      }
      oc = on;
    }

    l += dpp<ROR4>(l); l += dpp<ROR8>(l); l = xrow_sum(l);
    zacc += dpp<ROR4>(zacc); zacc += dpp<ROR8>(zacc); zacc = xrow_sum(zacc);
    const f32x2 zz = {dpp<0x00>(zacc), dpp<0xAA>(zacc)};
#pragma unroll
    for (int i = 0; i < 4; ++i) { acc[i][0] = half_sum(acc[i][0]); acc[i][1] = half_sum(acc[i][1]); acc[i] -= zz; }
    float* rec = lds + L_PART + wave * PREC;
    *reinterpret_cast<f32x4*>(rec + 4 * lane) = half ? f32x4{acc[0][1], acc[1][1], acc[2][1], acc[3][1]}
                                                     : f32x4{acc[0][0], acc[1][0], acc[2][0], acc[3][0]};
    if (lane == 0 || lane == 2) *reinterpret_cast<f32x2*>(rec + 2 * DH + lane) = f32x2{m, l};
    LDS_FENCE();
    if (lane == 0) __hip_atomic_fetch_add(lflag + F_DONE, 1, RLX_WG);
    return;
  }

  u64* cw = reinterpret_cast<u64*>(ws);
  unsigned* cw32 = reinterpret_cast<unsigned*>(ws);
  {
    u32x4 sv[16];
    if (lane < F_COUNT) lflag[lane] = 0;
    LDS_FENCE();
    asm volatile("s_barrier" ::: "memory");
#pragma unroll
    for (int i = 0; i < 8; ++i) {
      const int e = i * 64 + lane, w2 = (e >> 1) & 3, r2 = e >> 3;
      const int boff = wgoff + ((w2 & 1) << PW0) + ((w2 >> 1) << PW1) + scatter_round(r2);
      const size_t row = (size_t)hg * GTOK + (boff >> 9) + (e & 1) * 8;
      sv[2 * i] = __builtin_nontemporal_load(reinterpret_cast<const u32x4*>(vsc + row));
      sv[2 * i + 1] = __builtin_nontemporal_load(reinterpret_cast<const u32x4*>(vzr + row));
    }
#pragma unroll
    for (int i = 0; i < 8; ++i) {
      const int e = i * 64 + lane, w2 = (e >> 1) & 3, r2 = e >> 3;
      const int d = (w2 * NBW + r2) * 2 + (e & 1);
      reinterpret_cast<u32x4*>(lds + L_SZ)[d] = sv[2 * i];
      reinterpret_cast<u32x4*>(lds + L_SZ)[SW * NBW * 2 + d] = sv[2 * i + 1];
    }
    LDS_FENCE();
    if (lane == 0) __hip_atomic_store(lflag + F_STAGED, 1, RLX_WG);
  }
  u64 epoch = 0;
  {
    const u64 c0 = __builtin_amdgcn_s_memrealtime();
    const u64 hs_time = c0 + 86u * (unsigned)((b & 15) * 4 + (b >> 6));
    bool hs_done = false;
    int touched = 0;
    u64 rnd = (c0 >> RSH) + 1;
    int phase = 0;
    int kprev = -1; unsigned tprev = 0;
    unsigned tau = TAU0, origin = 0, h0 = TAU0, h1 = TAU0, h2 = TAU0;
    u64* tab = reinterpret_cast<u64*>(ws + WS_PACE);
    for (;;) {
      const int prog = lds_ld(lds, F_PROG);
      const bool ending = prog >= 2 * NBW - 4;
      if (touched < 3 && prog >= (touched + 1) * (NBW / 2) - 6 - (b & 15)) {
        ++touched;
        const int* pg = (touched == 1 ? qk : qv) + (size_t)hg * GTOK * DH + ((wgoff + scatter_round((touched & 1) * (NBW / 2))) >> 2);
        const int x = __builtin_nontemporal_load(pg + (lane & 15));
        asm volatile("" :: "v"(x));
        continue;
      }
      const u64 now = __builtin_amdgcn_s_memrealtime();
      if (!hs_done && (ending || now >= hs_time)) {
        const int wi = lane < 2 ? lane : CW_HEAD + head;
        u64 cwv = 0;
        if (lane < 3) cwv = __hip_atomic_load(cw + wi * CW_STRIDE, RLX_AGENT);
        epoch = ((u64)(unsigned)__builtin_amdgcn_readfirstlane((int)(cwv >> 32)) << 32) | (unsigned)__builtin_amdgcn_readfirstlane((int)cwv);
        const u64 tagv = (epoch * 0x9E3779B97F4A7C15ull + 0x632BE59BD9B4E019ull) << TAG_SHIFT;
        if (lane >= 1 && lane < 3 && (cwv >> TAG_SHIFT) != (tagv >> TAG_SHIFT))
          __hip_atomic_compare_exchange_strong(cw + wi * CW_STRIDE, &cwv, tagv, __ATOMIC_RELAXED, RLX_AGENT);
        asm volatile("s_waitcnt vmcnt(0)" ::: "memory");
        hs_done = true;
        continue;
      }
      if (ending) break;
      const u64 tb = rnd << RSH;
      u64* row = tab + (rnd & 63) * (40 * 32);
      if (phase == 0 && now >= tb) {
        if (b < 32) {
          unsigned ks = 0; int ts = 0; bool ok = true;
#pragma unroll
          for (int w2 = 0; w2 < SW; ++w2) {
            const u64 pg = lds_ld2(lds, F_PROG + 2 * w2);
            const int dt = (int)((unsigned)(pg >> 32) - ((unsigned)tb << 4));
            ok = ok && (unsigned)pg != 0u && dt > -30000 && dt < 30000;
            ks += (unsigned)pg; ts += dt;
          }
          if (ok && lane == 0)
            __hip_atomic_store(row + b * 32, (rnd << 28) | ((u64)(ks & 0x3ffu) << 18) | (u64)((unsigned)(ts + 131072) & 0x3ffffu), RLX_AGENT);
        }
        phase = 1;
      } else if (phase == 1 && now >= tb + 130) {
        if (b == 0) {
          u64 e = 0;
          if (lane < 32) e = __hip_atomic_load(row + lane * 32, RLX_AGENT);
          const bool val = lane < 32 && (e >> 28) == (rnd & 0xfffffffffull);
          const int n = __builtin_popcountll(__ballot(val));
          if (n >= 24) {
            int ks = val ? (int)((e >> 18) & 0x3ffu) : 0, ts = val ? (int)(e & 0x3ffffu) - 131072 : 0;
#pragma unroll
            for (int o = 1; o < 32; o <<= 1) { ks += __shfl_xor(ks, o); ts += __shfl_xor(ts, o); }
            ks = __builtin_amdgcn_readfirstlane(ks); ts = __builtin_amdgcn_readfirstlane(ts);
            const int kbar = (ks * 64) / (n * SW);
            const unsigned tbar = ((unsigned)tb << 4) + (unsigned)(ts / (n * SW));
            if (kprev >= 0 && kbar > kprev + 128) {
              unsigned slope = (unsigned)(((u64)(tbar - tprev) * 64u) / (unsigned)(kbar - kprev));
              const int lag = (int)(tbar - origin - (unsigned)(((u64)tau * (unsigned)kbar) >> 6));
              if (lag < MARGIN / 3) slope -= slope >> PROBE;
              h2 = h1; h1 = h0; h0 = slope;
              tau = min(h0, min(h1, h2));
              tau = tau < TAU_MIN ? TAU_MIN : tau > TAU_MAX ? TAU_MAX : tau;
            }
            kprev = kbar; tprev = tbar;
            origin = tbar - (unsigned)(((u64)tau * (unsigned)kbar) >> 6) - MARGIN;
            if (lane < 8) __hip_atomic_store(row + (32 + lane) * 32, ((rnd & 0xfffffull) << 44) | ((u64)tau << 32) | origin, RLX_AGENT);
          }
        }
        phase = 2;
      } else if (phase == 2 && now >= tb + 330) {
        const u64 e = __hip_atomic_load(row + (32 + (b & 7)) * 32, RLX_AGENT);
        const unsigned eo = (unsigned)e, et = (unsigned)(e >> 32) & 0xfffu;
        const int off = (int)(eo + (unsigned)lds_ld(lds, F_PROG) * et - ((unsigned)now << 4));
        if ((e >> 44) == (rnd & 0xfffffull) && et >= TAU_MIN && et <= TAU_MAX && off > -40000 && off < 40000 && lane == 0) lds_st2(lds, F_SCHED, eo, et);
        phase = 0; ++rnd;
      } else __builtin_amdgcn_s_sleep(1);
    }
    while (lds_ld(lds, F_DONE) != SW) {}
  }
  LDS_FENCE();
  const __amdgpu_buffer_rsrc_t rr = __builtin_amdgcn_make_buffer_rsrc((void*)(ws + WS_REC + (size_t)head * RPH * REC_BYTES), 0, RPH * REC_BYTES, 0x00020000);
  {
    const float* rb = lds + L_PART;
    float mw[SW], M = -INFINITY;
#pragma unroll
    for (int w2 = 0; w2 < SW; ++w2) { mw[w2] = rb[w2 * PREC + 2 * DH + 2 * half]; M = fmaxf(M, mw[w2]); }
    float L = 0.f; f32x4 o = {0.f, 0.f, 0.f, 0.f};
#pragma unroll
    for (int w2 = 0; w2 < SW; ++w2) {
      const float wt = __builtin_amdgcn_exp2f(mw[w2] - M);
      L = fmaf(wt, rb[w2 * PREC + 2 * DH + 2 * half + 1], L);
      o += wt * *reinterpret_cast<const f32x4*>(rb + w2 * PREC + 4 * lane);
    }
    const int ro = (b % RPH) * REC_BYTES;
    __builtin_amdgcn_raw_buffer_store_b128(__builtin_bit_cast(u32x4, o), rr, ro + lane * 16, 0, 16  );
    if (c == 0) __builtin_amdgcn_raw_buffer_store_b64(__builtin_bit_cast(u32x2, f32x2{M, L}), rr, ro + 1024 + half * 8, 0, 16);
  }
  asm volatile("s_waitcnt vmcnt(0)" ::: "memory");
  unsigned old = 0;
  if (lane == 0) old = __hip_atomic_fetch_add(cw32 + (CW_HEAD + head) * CW_STRIDE * 2, 1u, RLX_AGENT);
  if (((unsigned)__builtin_amdgcn_readfirstlane((int)old) & CNT_MASK) == RPH - 1) {
    int zoff = 0;
    asm volatile("" : "+v"(zoff) :: "memory");
    unsigned od = 0;
    if (lane == 0) od = __hip_atomic_fetch_add(cw32 + CW_DONE * CW_STRIDE * 2 + zoff, 1u, RLX_AGENT);
    f32x4 ov[RPH]; f32x2 ml[RPH];
#pragma unroll
    for (int i = 0; i < RPH; ++i) {
      ml[i] = __builtin_bit_cast(f32x2, __builtin_amdgcn_raw_buffer_load_b64(rr, i * REC_BYTES + 1024 + half * 8, 0, 16));
      ov[i] = __builtin_bit_cast(f32x4, __builtin_amdgcn_raw_buffer_load_b128(rr, i * REC_BYTES + lane * 16, 0, 16));
    }
    float M = -INFINITY;
#pragma unroll
    for (int i = 0; i < RPH; ++i) M = fmaxf(M, ml[i][0]);
    float L = 0.f; f32x4 o = {0.f, 0.f, 0.f, 0.f};
#pragma unroll
    for (int i = 0; i < RPH; ++i) {
      const float wt = __builtin_amdgcn_exp2f(ml[i][0] - M);
      L = fmaf(wt, ml[i][1], L);
      o += wt * ov[i];
    }
    const float inv = 1.0f / L;
    __hip_bfloat16 r4[4];
#pragma unroll
    for (int i = 0; i < 4; ++i) r4[i] = __float2bfloat16(o[i] * inv);
    *reinterpret_cast<u32x2*>(out + (head * 2 + half) * DH + 4 * c) = *reinterpret_cast<const u32x2*>(r4);
    asm volatile("" : "+v"(od));
    if (lane == 0 && (od & CNT_MASK) == HKV - 1) __hip_atomic_store(cw + CW_EPOCH * CW_STRIDE, epoch + 1, RLX_AGENT);
  }
}

}

extern "C" void kernel_launch(void* const* d_in, const int* in_sizes, int n_in, void* d_out, int out_size,
                              void* d_ws, size_t ws_size, hipStream_t stream) {
  if (n_in != 7 || in_sizes[0] != HQ * DH || in_sizes[1] != HKV * SKV * DH || in_sizes[2] != HKV * SKV * DH ||
      in_sizes[3] != HKV * NGK * DH || in_sizes[4] != HKV * NGK * DH || in_sizes[5] != HKV * SKV || in_sizes[6] != HKV * SKV ||
      out_size != HQ * DH || ws_size < WS_END)
    return;
  kv_decode_kernel<<<NWG, NTHR, 0, stream>>>((const unsigned short*)d_in[0], (const int*)d_in[1], (const int*)d_in[2],
                                             (const unsigned short*)d_in[3], (const unsigned short*)d_in[4],
                                             (const unsigned short*)d_in[5], (const unsigned short*)d_in[6],
                                             (unsigned char*)d_ws, (__hip_bfloat16*)d_out);
}
```
